```python
import torch

def _softmax(x, axis=-1):
    return torch.softmax(x, dim=axis)
def _gelu(x):
    return torch.nn.functional.gelu(x, approximate="tanh")
def _silu(x):
    return torch.nn.functional.silu(x)
def _sigmoid(x):
    return torch.sigmoid(x)
def _layernorm(x, axis=-1, eps=1e-5):
    m = x.mean(dim=axis, keepdim=True)
    v = x.var(dim=axis, keepdim=True, unbiased=False)
    return (x - m) / torch.sqrt(v + eps)
def _scatter_add(tgt, idx, src, axis=0):
    return tgt.clone().index_add_(axis, idx, src)
def _segment_sum(src, seg, n):
    out = torch.zeros((n,) + src.shape[1:], dtype=src.dtype)
    return out.index_add_(0, seg, src)
def _pad(x, pw):
    flat = tuple(v for pair in reversed(pw) for v in pair)
    return torch.nn.functional.pad(x, flat)

kernel_name = 'concat_heads__compressed_kv_cla__mha_block_cached__decode'

def setup_inputs():

    _w = (lambda a: (lambda t: t.to(torch.bfloat16) if t.is_floating_point() else t)(torch.as_tensor(a)))
    def _A_setup():
        B, H, Sq, Sk, D, M = 2, 32, 1, 8192, 128, 512
        Dc = 4 * D
        return {
            "x": _w((lambda *s: torch.randn(*s, dtype=torch.bfloat16) * 3.2)(B, Sq, M)),
            "c_kv": _w((lambda *s: torch.randn(*s, dtype=torch.bfloat16) * 0.1)(B, Sk, Dc)),
            "w_q": _w((lambda *s: torch.randn(*s, dtype=torch.bfloat16) * 6.4)(M, H, D) * (M ** -0.5)),
            "w_uk": _w((lambda *s: torch.randn(*s, dtype=torch.bfloat16) * 6.4)(Dc, H, D) * (Dc ** -0.5)),
            "w_uv": _w((lambda *s: torch.randn(*s, dtype=torch.bfloat16) * 0.1)(Dc, H, D) * (Dc ** -0.5)),
            "w_o": _w((lambda *s: torch.randn(*s, dtype=torch.bfloat16) * 0.1)(H, D, M) * ((H * D) ** -0.5)),
        }
    _A_in = _A_setup()

    def _B_setup():
        B, Sq, D = 2, 1, 512
        H, Sk, Dh = 32, 8192, 128
        return {
            "x": _w((lambda *s: torch.randn(*s, dtype=torch.bfloat16) * 1.6)(B, Sq, D)),
            "k_cache": _w((lambda *s: torch.randn(*s, dtype=torch.bfloat16) * 1.6)(B, H, Sk, Dh)),
            "v_cache": _w((lambda *s: torch.randn(*s, dtype=torch.bfloat16) * 0.05)(B, H, Sk, Dh)),
            "wq": _w((lambda *s: torch.randn(*s, dtype=torch.bfloat16) * 1.6)(D, H * Dh) * (D ** -0.5)),
            "wo": _w((lambda *s: torch.randn(*s, dtype=torch.bfloat16) * 0.05)(H * Dh, D) * ((H * Dh) ** -0.5)),
        }
    _B_in = _B_setup()

    return {
        "A_x": _A_in["x"],
        "A_c_kv": _A_in["c_kv"],
        "A_w_q": _A_in["w_q"],
        "A_w_uk": _A_in["w_uk"],
        "A_w_uv": _A_in["w_uv"],
        "A_w_o": _A_in["w_o"],
        "B_x": _B_in["x"],
        "B_k_cache": _B_in["k_cache"],
        "B_v_cache": _B_in["v_cache"],
        "B_wq": _B_in["wq"],
        "B_wo": _B_in["wo"],
    }

def reference(A_x, A_c_kv, A_w_q, A_w_uk, A_w_uv, A_w_o, B_x, B_k_cache, B_v_cache, B_wq, B_wo):
    def _A_ref(x, c_kv, w_q, w_uk, w_uv, w_o):
        B, Sq, M = x.shape[0], x.shape[1], x.shape[2]
        Sk = c_kv.shape[1]
        H, D = w_q.shape[1], w_q.shape[2]
        q = torch.einsum("bqm,mhd->bhqd", x, w_q)
        k = torch.einsum("bkc,chd->bhkd", c_kv, w_uk)
        v = torch.einsum("bkc,chd->bhkd", c_kv, w_uv)
        scores = torch.einsum("bhqd,bhkd->bhqk", q, k) * (D ** -0.5)
        off = Sk - Sq
        qi = torch.arange(Sq)[:, None]
        kj = torch.arange(Sk)[None, :]
        cmask = kj <= (qi + off)
        scores = torch.where(cmask[None, None], scores, -1e9)
        p = _softmax(scores, axis=-1)
        ctx = torch.einsum("bhqk,bhkd->bhqd", p, v)
        return torch.einsum("bhqd,hdm->bqm", ctx, w_o)

    def _B_ref(x, k_cache, v_cache, wq, wo):
        B, Sq, D = x.shape
        H, Dh = 32, 128
        q = (lambda x, *d: x.permute(*d))((x @ wq).reshape(B, Sq, H, Dh), 0, 2, 1, 3)
        scores = torch.einsum("bhqd,bhkd->bhqk", q, k_cache) * (Dh ** -0.5)
        p = _softmax(scores, axis=-1)
        o = torch.einsum("bhqk,bhkd->bhqd", p, v_cache)
        return (lambda x, *d: x.permute(*d))(o, 0, 2, 1, 3).reshape(B, Sq, H * Dh) @ wo

    a = _A_ref(A_x, A_c_kv, A_w_q, A_w_uk, A_w_uv, A_w_o)
    b = _B_ref(B_x, B_k_cache, B_v_cache, B_wq, B_wo)
    return torch.concatenate([a, b], axis=-1)
```

```cpp
#include <hip/hip_runtime.h>
#include <hip/hip_bf16.h>
#include <stdint.h>
typedef unsigned short u16;
#define ROUND_SCORES 1
__device__ __forceinline__ float bf2f(u16 v) { return __uint_as_float(((unsigned)v) << 16); }
__device__ __forceinline__ float rbf(float f) { return __bfloat162float(__float2bfloat16(f)); }

__global__ void k_proj(const u16* x, const u16* w, float* q) {
  int n = blockIdx.x * 256 + threadIdx.x; int b = blockIdx.y;
  float acc = 0.f;
  for (int m = 0; m < 512; ++m) acc += bf2f(x[b * 512 + m]) * bf2f(w[(size_t)m * 4096 + n]);
  q[b * 4096 + n] = rbf(acc);
}
__global__ void k_abs(const float* q, const u16* wuk, float* qabs) {
  int c = blockIdx.x * 256 + threadIdx.x; int h = blockIdx.y; int b = blockIdx.z;
  float acc = 0.f;
  for (int d = 0; d < 128; ++d) acc += q[b * 4096 + h * 128 + d] * bf2f(wuk[((size_t)c * 32 + h) * 128 + d]);
  qabs[(b * 32 + h) * 512 + c] = acc;
}
__global__ void k_scores(const float* q, const u16* K, float* s, int D, int perHead) {
  int k = blockIdx.x * 256 + threadIdx.x; int bh = blockIdx.y;
  const u16* kr = K + (perHead ? ((size_t)bh * 8192 + k) * D : ((size_t)(bh / 32) * 8192 + k) * D);
  float acc = 0.f;
  for (int d = 0; d < D; ++d) acc += q[bh * D + d] * bf2f(kr[d]);
  const float sc = 0.08838834764831845f;
  acc = rbf(rbf(acc) * sc);
  s[(size_t)bh * 8192 + k] = acc;
}
__global__ void k_softmax(float* s) {
  __shared__ float red[256];
  float* r = s + (size_t)blockIdx.x * 8192;
  float m = -INFINITY;
  for (int i = threadIdx.x; i < 8192; i += 256) m = fmaxf(m, r[i]);
  red[threadIdx.x] = m; __syncthreads();
  for (int o = 128; o > 0; o >>= 1) { if (threadIdx.x < o) red[threadIdx.x] = fmaxf(red[threadIdx.x], red[threadIdx.x + o]); __syncthreads(); }
  m = red[0]; __syncthreads();
  float l = 0.f;
  for (int i = threadIdx.x; i < 8192; i += 256) { float e = __expf(r[i] - m); r[i] = e; l += e; }
  red[threadIdx.x] = l; __syncthreads();
  for (int o = 128; o > 0; o >>= 1) { if (threadIdx.x < o) red[threadIdx.x] += red[threadIdx.x + o]; __syncthreads(); }
  l = red[0];
  for (int i = threadIdx.x; i < 8192; i += 256) r[i] = r[i] / l;
}
__global__ void k_pv(const float* p, const u16* V, float* opart, int D, int perHead) {
  int d = threadIdx.x; int sp = blockIdx.x; int bh = blockIdx.y; int NS = gridDim.x; int per = 8192 / NS;
  const u16* vb = V + (perHead ? ((size_t)bh * 8192) * D : ((size_t)(bh / 32) * 8192) * D);
  float acc = 0.f;
  for (int k = sp * per; k < (sp + 1) * per; ++k) acc += p[(size_t)bh * 8192 + k] * bf2f(vb[(size_t)k * D + d]);
  opart[((size_t)bh * NS + sp) * D + d] = acc;
}
__global__ void k_sum(const float* opart, float* o, int D, int NS, int rnd) {
  int d = threadIdx.x; int bh = blockIdx.x;
  float acc = 0.f;
  for (int s = 0; s < NS; ++s) acc += opart[((size_t)bh * NS + s) * D + d];
  o[bh * D + d] = rnd ? rbf(acc) : acc;
}
__global__ void k_uv(const float* cl, const u16* wuv, float* ctx) {
  int d = threadIdx.x; int bh = blockIdx.x; int h = bh & 31;
  float acc = 0.f;
  for (int c = 0; c < 512; ++c) acc += cl[bh * 512 + c] * bf2f(wuv[((size_t)c * 32 + h) * 128 + d]);
  ctx[bh * 128 + d] = rbf(acc);
}
__global__ void k_out(const float* o, const u16* w, __hip_bfloat16* out, int off) {
  int m = blockIdx.x * 64 + threadIdx.x; int b = blockIdx.y;
  float acc = 0.f;
  for (int n = 0; n < 4096; ++n) acc += o[b * 4096 + n] * bf2f(w[(size_t)n * 512 + m]);
  out[b * 1024 + off + m] = __float2bfloat16(acc);
}
extern "C" void kernel_launch(void* const* d_in, const int* in_sizes, int n_in, void* d_out, int out_size, void* d_ws, size_t ws_size, hipStream_t stream) {
  const u16 *Ax = (const u16*)d_in[0], *Ac = (const u16*)d_in[1], *Awq = (const u16*)d_in[2], *Awuk = (const u16*)d_in[3], *Awuv = (const u16*)d_in[4], *Awo = (const u16*)d_in[5];
  const u16 *Bx = (const u16*)d_in[6], *Bk = (const u16*)d_in[7], *Bv = (const u16*)d_in[8], *Bwq = (const u16*)d_in[9], *Bwo = (const u16*)d_in[10];
  float* ws = (float*)d_ws;
  float* qA = ws; float* qB = qA + 8192; float* qabs = qB + 8192; float* sA = qabs + 32768; float* sB = sA + 64 * 8192;
  float* pa = sB + 64 * 8192; float* pb = pa + 64 * 32 * 512; float* cl = pb + 64 * 32 * 128; float* oB = cl + 64 * 512; float* ctx = oB + 8192;
  __hip_bfloat16* out = (__hip_bfloat16*)d_out;
  k_proj<<<dim3(16, 2), 256, 0, stream>>>(Ax, Awq, qA);
  k_proj<<<dim3(16, 2), 256, 0, stream>>>(Bx, Bwq, qB);
  k_abs<<<dim3(2, 32, 2), 256, 0, stream>>>(qA, Awuk, qabs);
  k_scores<<<dim3(32, 64), 256, 0, stream>>>(qabs, Ac, sA, 512, 0);
  k_scores<<<dim3(32, 64), 256, 0, stream>>>(qB, Bk, sB, 128, 1);
  k_softmax<<<128, 256, 0, stream>>>(sA);
  k_pv<<<dim3(32, 64), 512, 0, stream>>>(sA, Ac, pa, 512, 0);
  k_pv<<<dim3(32, 64), 128, 0, stream>>>(sB, Bv, pb, 128, 1);
  k_sum<<<64, 512, 0, stream>>>(pa, cl, 512, 32, 0);
  k_sum<<<64, 128, 0, stream>>>(pb, oB, 128, 32, 1);
  k_uv<<<64, 128, 0, stream>>>(cl, Awuv, ctx);
  k_out<<<dim3(8, 2), 64, 0, stream>>>(ctx, Awo, out, 0);
  k_out<<<dim3(8, 2), 64, 0, stream>>>(oB, Bwo, out, 512);
}
```

```cpp
#include <hip/hip_runtime.h>
#include <hip/hip_bf16.h>
#include <stdint.h>

typedef unsigned short u16;
typedef unsigned int u32;
typedef unsigned long long u64;
typedef u32 u32x4 __attribute__((ext_vector_type(4)));
typedef u32 u32x2 __attribute__((ext_vector_type(2)));
typedef float f32x4 __attribute__((ext_vector_type(4)));
typedef float f32x2 __attribute__((ext_vector_type(2)));
typedef short s16x8 __attribute__((ext_vector_type(8)));
typedef short s16x4 __attribute__((ext_vector_type(4)));
typedef __bf16 bf16x2 __attribute__((ext_vector_type(2)));
#define LAS __attribute__((address_space(3)))
#define GAS __attribute__((address_space(1)))
#define RLX_AGENT __ATOMIC_RELAXED, __HIP_MEMORY_SCOPE_AGENT

constexpr int WIN = 4;
constexpr int PRE = 28;
constexpr int EARLY = 2;
constexpr int LOCK = 4;
#define NSS 35

constexpr int NWG = 256, NBW = 8, NAW = 4, NTHR = (NBW + NAW) * 64;
constexpr float SCALE = 0.08838834764831845f;
constexpr float LOG2E = 1.4426950408889634f;
constexpr u32 SENT = 0xFFFFFFFFu;
constexpr u32 SPIN_CAP = 1u << 16;

constexpr u32 WS_EPOCH = 0;
constexpr u32 WS_FC = 4096;
constexpr u32 WS_F1 = 8192;
constexpr u32 WS_F2 = 12288;
constexpr u32 WS_CTR = 16384;
constexpr u32 WS_OBS = 32768;
constexpr u32 WS_PST = 49152;
constexpr u32 WS_QX = 65536;
constexpr u32 WS_QG = WS_QX + 131072;
constexpr u32 WS_YA = WS_QG + 131072;
constexpr u32 WS_YB = WS_YA + 262144;
constexpr u32 WS_OB = WS_YB + 262144;
constexpr u32 WS_PART = WS_OB + 1048576;
constexpr u32 WS_QGP = WS_PART + 2097152;
constexpr u32 WS_END = WS_QGP + 524288;

constexpr int RS = 1056;
constexpr int L_X = 0;
constexpr int L_ST0 = 96 * RS;
constexpr int L_FB = 0;
constexpr int L_WO = 65536;
constexpr int L_SX = 135168;
constexpr int L_P = L_SX + 18432;
constexpr int L_AL = L_P + 2560;
constexpr int L_XV = L_AL + 128;
constexpr int L_SYNC = L_XV + 4096;
constexpr int L_QB = L_SYNC + 64;
constexpr int L_END = L_QB + 512;
static_assert(L_END <= 163840, "LDS");

struct Params {
  const u16* Ax; const u16* Ac; const u16* Awq; const u16* Awuk; const u16* Awuv; const u16* Awo;
  const u16* Bx; const u16* Bk; const u16* Bv; const u16* Bwq; const u16* Bwo;
  u16* out; unsigned char* ws;
};

__device__ __forceinline__ float asf(u32 v) { return __builtin_bit_cast(float, v); }
__device__ __forceinline__ u32 asu(float v) { return __builtin_bit_cast(u32, v); }
__device__ __forceinline__ float bflo(u32 v) { return asf(v << 16); }
__device__ __forceinline__ float bfhi(u32 v) { return asf(v & 0xffff0000u); }
__device__ __forceinline__ u32 cvtpk(float lo, float hi) {
  const f32x2 v = {lo, hi}; return __builtin_bit_cast(u32, __builtin_convertvector(v, bf16x2));
}
__device__ __forceinline__ float rbf(float x) { return bflo(cvtpk(x, x)); }
__device__ __forceinline__ float dot2(u32 k, u32 q, float acc) {
  return __builtin_amdgcn_fdot2_f32_bf16(__builtin_bit_cast(bf16x2, k), __builtin_bit_cast(bf16x2, q), acc, false);
}
template <int CTRL> __device__ __forceinline__ float dpp(float x) {
  return asf(__builtin_amdgcn_mov_dpp(__builtin_bit_cast(int, x), CTRL, 0xf, 0xf, true));
}
constexpr int XOR1 = 0xB1, XOR2 = 0x4E, XOR7 = 0x141, ROR8 = 0x128, ROR4 = 0x124, BCAST = 0x150;
__device__ __forceinline__ float xrow_sum(float x) {
  auto s = __builtin_amdgcn_permlane16_swap(asu(x), asu(x), false, false);
  x = asf((u32)s[0]) + asf((u32)s[1]);
  auto t = __builtin_amdgcn_permlane32_swap(asu(x), asu(x), false, false);
  return asf((u32)t[0]) + asf((u32)t[1]);
}
__device__ __forceinline__ float xrow_max(float x) {
  auto s = __builtin_amdgcn_permlane16_swap(asu(x), asu(x), false, false);
  x = fmaxf(asf((u32)s[0]), asf((u32)s[1]));
  auto t = __builtin_amdgcn_permlane32_swap(asu(x), asu(x), false, false);
  return fmaxf(asf((u32)t[0]), asf((u32)t[1]));
}
__device__ __forceinline__ float row_sum16(float x) {
  x += dpp<XOR1>(x); x += dpp<XOR2>(x); x += dpp<XOR7>(x); x += dpp<ROR8>(x); return x;
}
__device__ __forceinline__ float row_max16(float x) {
  x = fmaxf(x, dpp<XOR1>(x)); x = fmaxf(x, dpp<XOR2>(x)); x = fmaxf(x, dpp<ROR4>(x)); x = fmaxf(x, dpp<ROR8>(x)); return x;
}
__device__ __forceinline__ float tr_reduce16(float (&x)[16], int c) {
#define TR_STEP(HALF, CTRL, BIT) _Pragma("unroll") for (int h_ = 0; h_ < (HALF); ++h_) { \
    const float lo_ = x[h_], hi_ = x[h_ + (HALF)]; const float t1_ = lo_ + dpp<CTRL>(lo_), t2_ = hi_ + dpp<CTRL>(hi_); \
    x[h_] = (c & (BIT)) ? t2_ : t1_; }
  TR_STEP(8, ROR8, 8) TR_STEP(4, XOR7, 4) TR_STEP(2, XOR2, 2) TR_STEP(1, XOR1, 1)
#undef TR_STEP
  return x[0];
}
constexpr int NT = 2;
__device__ __forceinline__ __amdgpu_buffer_rsrc_t mkrs(const void* p, u32 bytes) { return __builtin_amdgcn_make_buffer_rsrc((void*)p, 0, bytes, 0x00020000); }
__device__ __forceinline__ u32x4 ldnt(__amdgpu_buffer_rsrc_t r, u32 voff, u32 soff) { return __builtin_amdgcn_raw_buffer_load_b128(r, voff, soff, NT); }
__device__ __forceinline__ f32x4 mfma16(u32x4 a, u32x4 b, f32x4 c) {
  return __builtin_amdgcn_mfma_f32_16x16x32_bf16(__builtin_bit_cast(s16x8, a), __builtin_bit_cast(s16x8, b), c, 0, 0, 0);
}
__device__ __forceinline__ u32x2 trread(LAS unsigned char* p) {
  return __builtin_bit_cast(u32x2, __builtin_amdgcn_ds_read_tr16_b64_v4i16((LAS s16x4*)p));
}
__device__ __forceinline__ void glds16(const void* sbase, u32 voff, u32 lds_dst) {
  u32 keep;
  const u64 a_ = (u64)sbase;
  const u64 sb_ = ((u64)(u32)__builtin_amdgcn_readfirstlane((u32)(a_ >> 32)) << 32) | (u32)__builtin_amdgcn_readfirstlane((u32)a_);
  lds_dst = __builtin_amdgcn_readfirstlane(lds_dst);
  asm volatile("s_mov_b32 %0, m0\n\ts_mov_b32 m0, %3\n\ts_nop 4\n\tglobal_load_lds_dwordx4 %1, %2 nt\n\ts_mov_b32 m0, %0"
               : "=&s"(keep) : "v"(voff), "s"(sb_), "s"(lds_dst) : "memory");
}
__device__ __forceinline__ void glds16d(const void* sbase, u32 voff, u32 lds_dst) {
  u32 keep;
  const u64 a_ = (u64)sbase;
  const u64 sb_ = ((u64)(u32)__builtin_amdgcn_readfirstlane((u32)(a_ >> 32)) << 32) | (u32)__builtin_amdgcn_readfirstlane((u32)a_);
  lds_dst = __builtin_amdgcn_readfirstlane(lds_dst);
  asm volatile("s_mov_b32 %0, m0\n\ts_mov_b32 m0, %3\n\ts_nop 4\n\tglobal_load_lds_dwordx4 %1, %2\n\ts_mov_b32 m0, %0"
               : "=&s"(keep) : "v"(voff), "s"(sb_), "s"(lds_dst) : "memory");
}
#define DRAIN() asm volatile("s_waitcnt vmcnt(0)" ::: "memory")
#define LDSWAIT() asm volatile("s_waitcnt lgkmcnt(0)" ::: "memory")
#define SCHED() __builtin_amdgcn_sched_barrier(0)
#define PACE() asm volatile("s_waitcnt vmcnt(%0)" :: "i"(WIN) : "memory")
constexpr int SC1 = 16;
template <int U> __device__ __forceinline__ void fma8(float (&acc)[8], u32x4 v, float psrc) {
  const float p = dpp<BCAST + U>(psrc);
#pragma unroll
  for (int k = 0; k < 4; ++k) { acc[2 * k] = fmaf(p, bflo(v[k]), acc[2 * k]); acc[2 * k + 1] = fmaf(p, bfhi(v[k]), acc[2 * k + 1]); }
}
__device__ __forceinline__ int fresh_lane() {
  int z = 0; asm volatile("" : "+v"(z));
  return __builtin_amdgcn_mbcnt_hi(~0u, __builtin_amdgcn_mbcnt_lo(~0u, z));
}
__device__ __forceinline__ const unsigned char* sptr(const void* p) {
  const u64 a = (u64)p;
  return (const unsigned char*)(((u64)(u32)__builtin_amdgcn_readfirstlane((u32)(a >> 32)) << 32) | (u32)__builtin_amdgcn_readfirstlane((u32)a));
}
__device__ __forceinline__ u32 san(float f) { const u32 u = asu(f); return u == SENT ? 0x7FC00000u : u; }

template <int NF, int MASK, int SLP = 2> __device__ __forceinline__ void poll_flags(const unsigned char* base, u64 tag, int lane) {
  for (u32 spins = 0; spins < SPIN_CAP; ++spins) {
    bool ok = true;
#pragma unroll
    for (int i = 0; i < NF; ++i) ok &= (__hip_atomic_load((GAS u64*)(base) + i * 64 + (lane & MASK), RLX_AGENT) == tag);
    if (__all(ok)) break;
    __builtin_amdgcn_s_sleep(SLP);
  }
  __builtin_amdgcn_fence(__ATOMIC_ACQUIRE, "wavefront");
  asm volatile("" ::: "memory");
}
__device__ __forceinline__ u32x4 ldsc(__amdgpu_buffer_rsrc_t r, u32 voff, u32 soff) { return __builtin_amdgcn_raw_buffer_load_b128(r, voff, soff, SC1); }
__device__ __forceinline__ void stsc(f32x4 v, __amdgpu_buffer_rsrc_t r, u32 voff) { __builtin_amdgcn_raw_buffer_store_b128(__builtin_bit_cast(u32x4, v), r, voff, 0, SC1); }

__device__ __forceinline__ void lds_wait_ge(LAS unsigned char* L, int off, u32 want) {
  LAS u32* fl = (LAS u32*)(L + off);
  for (u32 spins = 0; spins < (1u << 21); ++spins) {
    if (__builtin_amdgcn_readfirstlane(__hip_atomic_load(fl, __ATOMIC_RELAXED, __HIP_MEMORY_SCOPE_WORKGROUP)) >= want) break;
    __builtin_amdgcn_s_sleep(1);
  }
  asm volatile("" ::: "memory");
}
__device__ __forceinline__ f32x4 san4(f32x4 v) { return f32x4{asf(san(v[0])), asf(san(v[1])), asf(san(v[2])), asf(san(v[3]))}; }
__device__ __forceinline__ bool valid4(u32x4 v) { return v[0] != SENT && v[1] != SENT && v[2] != SENT && v[3] != SENT; }
__device__ __forceinline__ u64 read_tag(const unsigned char* ws) {
  const u64 ep = __hip_atomic_load((GAS u64*)(ws + WS_EPOCH), RLX_AGENT);
  return (((u64)(u32)__builtin_amdgcn_readfirstlane((u32)(ep >> 32)) << 32) | (u32)__builtin_amdgcn_readfirstlane((u32)ep)) + 1;
}
#define BAR() do { LDSWAIT(); __builtin_amdgcn_s_barrier(); asm volatile("" ::: "memory"); } while (0)
#define PIN4(a, i) asm volatile("" : "+v"((a)[(i)]), "+v"((a)[(i) + 1]), "+v"((a)[(i) + 2]), "+v"((a)[(i) + 3]))

__device__ __forceinline__ void lds_set(LAS unsigned char* L, int off, int lane) {
  LDSWAIT();
  if (lane == 0) __hip_atomic_store((LAS u32*)(L + off), 1u, __ATOMIC_RELAXED, __HIP_MEMORY_SCOPE_WORKGROUP);
}
__device__ __forceinline__ u32 lds_add(LAS unsigned char* L, int off, int lane) {
  LDSWAIT();
  u32 v = 0;
  if (lane == 0) v = __hip_atomic_fetch_add((LAS u32*)(L + off), 1u, __ATOMIC_RELAXED, __HIP_MEMORY_SCOPE_WORKGROUP);
  v = __builtin_amdgcn_readfirstlane(v);
  asm volatile("" ::: "memory");
  return v;
}
__device__ __forceinline__ u64 stage0(const Params& p, LAS unsigned char* L, __amdgpu_buffer_rsrc_t rs, int bid, int hw, int lane) {
  const int jj = bid >> 3, h = (bid & 7) * 4 + (jj >> 3), j8 = jj & 7, kq = j8 >> 1, dh = j8 & 1;
  const int r = lane >> 4, c = lane & 15, mat = hw >> 1, hh = hw & 1;
  const unsigned char* wp = (const unsigned char*)(mat ? p.Awq : p.Bwq) + (size_t)(128 * kq + 64 * hh) * 8192 + h * 256 + dh * 128;
  const u32 vo = (lane >> 3) * 8192 + (lane & 7) * 16, lb = (u32)(size_t)(L + L_ST0) + hw * 8192;
#pragma unroll
  for (int P = 0; P < 8; ++P) glds16(wp + (size_t)P * 65536, vo, lb + P * 1024);
  const u64 ep = __hip_atomic_load((GAS u64*)(p.ws + WS_EPOCH), RLX_AGENT);
  const u16* xp = (mat ? p.Ax : p.Bx) + (c & 1) * 512 + 128 * kq + 8 * r;
  u32x4 xf[4];
#pragma unroll
  for (int kk = 0; kk < 4; ++kk) xf[kk] = *(const u32x4*)(xp + 32 * kk);
  if (hw == 0 && j8 == 0 && h < 4 && lane < 2) __hip_atomic_store((GAS u32*)(p.ws + WS_CTR + (2 * h + lane) * 256), 0u, RLX_AGENT);
  SCHED();
  BAR();
  DRAIN();
  lds_add(L, L_SYNC + 24 + mat * 4, lane);
  lds_wait_ge(L, L_SYNC + 24 + mat * 4, 2u);
  const u64 tag = (((u64)(u32)__builtin_amdgcn_readfirstlane((u32)(ep >> 32)) << 32) | (u32)__builtin_amdgcn_readfirstlane((u32)ep)) + 1;
  LAS unsigned char* tb = L + L_ST0 + mat * 16384 + (8 * r + (c >> 2)) * 128 + hh * 64 + (c & 3) * 8;
  f32x4 d0 = {0.f, 0.f, 0.f, 0.f}, d1 = d0;
#pragma unroll
  for (int kk = 0; kk < 4; ++kk) {
    const u32x2 a0 = trread(tb + kk * 4096), a1 = trread(tb + 512 + kk * 4096), a2 = trread(tb + 32 + kk * 4096), a3 = trread(tb + 544 + kk * 4096);
    d0 = mfma16(u32x4{a0[0], a0[1], a1[0], a1[1]}, xf[kk], d0);
    d1 = mfma16(u32x4{a2[0], a2[1], a3[0], a3[1]}, xf[kk], d1);
  }
  const int ts = (c >> 2) & 1, ep2 = (c >> 1) & 1;
  const f32x4 v = ts ? d1 : d0;
  const u32 v0 = (asu(ep2 ? v[2] : v[0]) + 0x8u) >> 4, v1 = (asu(ep2 ? v[3] : v[1]) + 0x8u) >> 4;
  const u32 tlo = (u32)(tag << 28), thi = (u32)(tag >> 4);
  if (c < 8) __builtin_amdgcn_raw_buffer_store_b128(u32x4{tlo | v0, thi, tlo | v1, thi}, rs,
      WS_QGP + ((((mat * 32 + h) * 2 + (c & 1)) * 4 + kq) * 128 + 64 * dh + 32 * hh + 16 * ts + 4 * r + 2 * ep2) * 8, 0, SC1);
  return tag;
}
__device__ __forceinline__ u32 gather_qp(__amdgpu_buffer_rsrc_t rs, int mat, int h, int b, u64 tag, int lane) {
  const u32 tlo = (u32)(tag << 28), thi = (u32)(tag >> 4);
  const u32 off = WS_QGP + ((mat * 32 + h) * 2 + b) * 4096 + lane * 16;
  u32x4 g[4];
  u32 pend = 0xfu;
  {
    const u32 io = WS_QGP + ((mat * 32 + h) * 2 + b) * 4096 + (lane & 15) * 256;
    for (u32 spins = 0; spins < SPIN_CAP; ++spins) {
      const u32x2 v = __builtin_amdgcn_raw_buffer_load_b64(rs, io, 0, SC1);
      if (__all((v[0] & 0xf0000000u) == tlo && v[1] == thi)) break;
      __builtin_amdgcn_s_sleep(4);
      asm volatile("" ::: "memory");
    }
  }
  for (u32 spins = 0; spins < SPIN_CAP && pend; ++spins) {
#pragma unroll
    for (int i = 0; i < 4; ++i) if (pend & (1u << i)) g[i] = ldsc(rs, off, i * 1024);
#pragma unroll
    for (int i = 0; i < 4; ++i) if (pend & (1u << i)) {
      if (__all((g[i][0] & 0xf0000000u) == tlo && g[i][1] == thi && (g[i][2] & 0xf0000000u) == tlo && g[i][3] == thi)) pend &= ~(1u << i);
    }
    asm volatile("" ::: "memory");
  }
  float a = 0.f, bsum = 0.f;
#pragma unroll
  for (int i = 0; i < 4; ++i) { a += asf(g[i][0] << 4); bsum += asf(g[i][2] << 4); }
  return cvtpk(a, bsum);
}
template <int NV> __device__ __forceinline__ void gather_q(LAS unsigned char* dst, __amdgpu_buffer_rsrc_t rs, u32 off, u32 stride, u64 tag, int lane) {
  const u32 tlo = (u32)(tag << 16), thi = (u32)(tag >> 16);
  u32x4 g[NV];
  u32 pend = (1u << NV) - 1u;
  for (u32 spins = 0; spins < SPIN_CAP && pend; ++spins) {
#pragma unroll
    for (int i = 0; i < NV; ++i) if (pend & (1u << i)) g[i] = ldsc(rs, off + lane * 16, i * stride);
#pragma unroll
    for (int i = 0; i < NV; ++i) if (pend & (1u << i)) {
      if (__all((g[i][0] & 0xffff0000u) == tlo && g[i][1] == thi && (g[i][2] & 0xffff0000u) == tlo && g[i][3] == thi)) pend &= ~(1u << i);
    }
    asm volatile("" ::: "memory");
  }
#pragma unroll
  for (int i = 0; i < NV; ++i) *(LAS u32*)(dst + i * 256 + lane * 4) = (g[i][0] & 0xffffu) | (g[i][2] << 16);
}
template <int SLP> __device__ __forceinline__ void final_reduce(const Params& p, __amdgpu_buffer_rsrc_t rs, u32 base, int rb, int ri, int ocol, int lane) {
  const int pp = lane >> 2, cq = lane & 3;
  const u32 yo = base + pp * 4096 + rb * 2048 + (16 * ri + 4 * cq) * 4;
  u32x4 ya[4];
  for (u32 spins = 0; spins < SPIN_CAP; ++spins) {
#pragma unroll
    for (int i = 0; i < 4; ++i) ya[i] = ldsc(rs, yo, i * 65536);
    if (__all(valid4(ya[0]) && valid4(ya[1]) && valid4(ya[2]) && valid4(ya[3]))) break;
    if (SLP) __builtin_amdgcn_s_sleep(SLP);
    asm volatile("" ::: "memory");
  }
  f32x4 o = (__builtin_bit_cast(f32x4, ya[0]) + __builtin_bit_cast(f32x4, ya[1])) + (__builtin_bit_cast(f32x4, ya[2]) + __builtin_bit_cast(f32x4, ya[3]));
#pragma unroll
  for (int e = 0; e < 4; ++e) { float x = o[e]; x += dpp<ROR4>(x); x += dpp<ROR8>(x); o[e] = xrow_sum(x); }
  if (lane < 4) *(u32x2*)(p.out + rb * 1024 + ocol + 16 * ri + 4 * cq) = u32x2{cvtpk(o[0], o[1]), cvtpk(o[2], o[3])};
}
__device__ __forceinline__ void fma8p(float (&acc)[8], u32x4 v, float p) {
#pragma unroll
  for (int k = 0; k < 4; ++k) { acc[2 * k] = fmaf(p, bflo(v[k]), acc[2 * k]); acc[2 * k + 1] = fmaf(p, bfhi(v[k]), acc[2 * k + 1]); }
}

constexpr int POOL0 = 3 * NSS, POOLN = 128 - POOL0;
#define UPAIR(v_) ((u32)((i8 & 1) * 32 + ((v_) & 7u) * 4 + (i8 >> 1)))
#define UNIT_ISSUE(u_, v_, slot_) do { \
    BAR(); \
    v_ = __builtin_amdgcn_readfirstlane(*(LAS u32*)(L + L_SYNC + 32 + (slot_) * 4)); \
    const bool ok_ = v_ < (u32)(8 * POOLN);                            \
    const u32 so_ = ok_ ? UPAIR(v_) * 2097152u + (POOL0 + (v_ >> 3)) * 16384u : 0u; \
    const u32 vo_ = ok_ ? w * 2048 + lane * 16 : 0x8000000u; \
    u_[0] = ldnt(kg, vo_, so_); u_[1] = ldnt(kg, vo_ + 1024, so_); u_[2] = ldnt(vg, vo_, so_); u_[3] = ldnt(vg, vo_ + 1024, so_); \
    SCHED(); } while (0)
__device__ __forceinline__ void dyn_flush(LAS unsigned char* L, float (&a)[8], float m, float lr, int fbi, int w, int lane) {
  const int r = lane >> 4, c = lane & 15;
#pragma unroll
  for (int k = 0; k < 8; ++k) a[k] = xrow_sum(a[k]);
  const float lw = xrow_sum(lr);
  LAS unsigned char* fb = L + L_FB + fbi * 4160;
  if (r < 2) *(LAS f32x4*)(fb + w * 512 + c * 32 + r * 16) = r == 0 ? f32x4{a[0], a[1], a[2], a[3]} : f32x4{a[4], a[5], a[6], a[7]};
  if (lane == 32) *(LAS u32x2*)(fb + 4096 + w * 8) = u32x2{asu(m), asu(lw)};
}
#define UNIT_PROC(u_, v_) do { const u32x4 q_ = *(LAS u32x4*)(L + L_P + ((v_) & 7u) * 256 + (lane & 15) * 16); \
    float d0 = dot2(u_[0][0], q_[0], 0.f); d0 = dot2(u_[0][1], q_[1], d0); d0 = dot2(u_[0][2], q_[2], d0); d0 = dot2(u_[0][3], q_[3], d0); \
    float d1 = dot2(u_[1][0], q_[0], 0.f); d1 = dot2(u_[1][1], q_[1], d1); d1 = dot2(u_[1][2], q_[2], d1); d1 = dot2(u_[1][3], q_[3], d1); \
    d0 = row_sum16(d0); d1 = row_sum16(d1); \
    const u32 r01 = cvtpk(d0, d1); const u32 t01 = cvtpk(bflo(r01) * SCALE, bfhi(r01) * SCALE); \
    const float s0 = bflo(t01), s1 = bfhi(t01); mu_ = xrow_max(fmaxf(s0, s1)); \
    const float p0 = __builtin_amdgcn_exp2f((s0 - mu_) * LOG2E), p1 = __builtin_amdgcn_exp2f((s1 - mu_) * LOG2E); \
    _Pragma("unroll") for (int k = 0; k < 8; ++k) ax_[k] = 0.f; \
    fma8p(ax_, u_[2], p0); fma8p(ax_, u_[3], p1); lu_ = p0 + p1; \
    asm volatile("" : "+v"(ax_[0]), "+v"(ax_[1]), "+v"(ax_[2]), "+v"(ax_[3]), "+v"(ax_[4]), "+v"(ax_[5]), "+v"(ax_[6]), "+v"(ax_[7]) :: "memory"); } while (0)
#define DYN_LOOP() do { float mu_, lu_, ax_[8]; \
    _Pragma("unroll 1") while (va < (u32)(8 * POOLN)) { \
      UNIT_PROC(ua, va); UNIT_ISSUE(ua, va, 0); dyn_flush(L, ax_, mu_, lu_, 0, w, lane); \
      UNIT_PROC(ub, vb_); UNIT_ISSUE(ub, vb_, 1); dyn_flush(L, ax_, mu_, lu_, 1, w, lane); } \
    BAR(); } while (0)
__device__ __forceinline__ void dyn_publish(LAS unsigned char* L, __amdgpu_buffer_rsrc_t rs, int lane, int fbi, u32 pr, u32 j) {
  LAS unsigned char* fb = L + L_FB + fbi * 4160;
  const int hh = lane >> 5, l32 = lane & 31;
  const f32x4 s0 = *(LAS f32x4*)(fb + 4096 + hh * 32), s1 = *(LAS f32x4*)(fb + 4096 + hh * 32 + 16);
  f32x4 pv[4];
#pragma unroll
  for (int i = 0; i < 4; ++i) pv[i] = *(LAS f32x4*)(fb + (4 * hh + i) * 512 + l32 * 16);
  float M = fmaxf(fmaxf(s0[0], s0[2]), fmaxf(s1[0], s1[2]));
  { auto t = __builtin_amdgcn_permlane32_swap(asu(M), asu(M), false, false); M = fmaxf(asf((u32)t[0]), asf((u32)t[1])); }
  const float f0 = __builtin_amdgcn_exp2f((s0[0] - M) * LOG2E), f1 = __builtin_amdgcn_exp2f((s0[2] - M) * LOG2E);
  const float f2 = __builtin_amdgcn_exp2f((s1[0] - M) * LOG2E), f3 = __builtin_amdgcn_exp2f((s1[2] - M) * LOG2E);
  f32x4 o = (f0 * pv[0] + f1 * pv[1]) + (f2 * pv[2] + f3 * pv[3]);
  float Ls = (f0 * s0[1] + f1 * s0[3]) + (f2 * s1[1] + f3 * s1[3]);
#pragma unroll
  for (int e = 0; e < 4; ++e) { auto t = __builtin_amdgcn_permlane32_swap(asu(o[e]), asu(o[e]), false, false); o[e] = asf((u32)t[0]) + asf((u32)t[1]); }
  { auto t = __builtin_amdgcn_permlane32_swap(asu(Ls), asu(Ls), false, false); Ls = asf((u32)t[0]) + asf((u32)t[1]); }
  const u32 oo = pr < 64u ? WS_OB + (pr * 32 + j) * 512 : WS_END, os = pr < 64u ? WS_OBS + (pr * 32 + j) * 8 : WS_END;
  const f32x4 ov = san4(o);
  const u32x4 pk = lane < 32 ? __builtin_bit_cast(u32x4, ov) : u32x4{san(M), san(Ls), 0u, 0u};
  __builtin_amdgcn_raw_buffer_store_b128(pk, rs, lane < 32 ? oo + lane * 16 : WS_END, 0, SC1);
  __builtin_amdgcn_raw_buffer_store_b64(u32x2{pk[0], pk[1]}, rs, lane == 32 ? os : WS_END, 0, SC1);
}
#define TICKET(t_) do { t_ = (u32)__builtin_amdgcn_raw_ptr_buffer_atomic_add_i32(1, rs, cto, 0, 0); } while (0)
__device__ __forceinline__ void dyn_side(LAS unsigned char* L, __amdgpu_buffer_rsrc_t rs, int lane, int i8, u32 cto, u32 tka, u32 tkb, u32 pprev, u32 jprev) {
#define POST(t_, v_, slot_) do { v_ = __builtin_amdgcn_readfirstlane(t_); if (lane == 0) *(LAS u32*)(L + L_SYNC + 32 + (slot_) * 4) = v_; BAR(); TICKET(t_); } while (0)
#define PAD2() do { __builtin_amdgcn_raw_buffer_store_b32(0u, rs, WS_END, 0, SC1); __builtin_amdgcn_raw_buffer_store_b32(0u, rs, WS_END + 64, 0, SC1); } while (0)
  u32 va, vb_;
  POST(tka, va, 0); PAD2();
  POST(tkb, vb_, 1); PAD2();
#pragma unroll 1
  while (va < (u32)(8 * POOLN)) {
    const u32 pa = UPAIR(va), ja = 3u + (va >> 3);
    POST(tka, va, 0);
    dyn_publish(L, rs, lane, 1, pprev, jprev);
    pprev = vb_ < (u32)(8 * POOLN) ? UPAIR(vb_) : 64u; jprev = 3u + (vb_ >> 3);
    POST(tkb, vb_, 1);
    dyn_publish(L, rs, lane, 0, pa, ja);
  }
  BAR();
  dyn_publish(L, rs, lane, 1, pprev, jprev);
}

__device__ void code_end_marker();
__global__ void __launch_bounds__(NTHR, 3) fused_decode_kernel(Params p) {
  __shared__ __attribute__((aligned(16))) unsigned char smem[163840];
  LAS unsigned char* L = (LAS unsigned char*)smem;
  const int wave = __builtin_amdgcn_readfirstlane(threadIdx.x >> 6);
  const int bid = blockIdx.x, xcd = bid & 7;
  const __amdgpu_buffer_rsrc_t rs = __builtin_amdgcn_make_buffer_rsrc(p.ws, 0, WS_END, 0x00020000);
  const u32 xbase = (u32)(size_t)(L + L_X);

  if (bid < 8 && wave < 8) {
    const unsigned char* pc = (const unsigned char*)(__builtin_amdgcn_s_getpc() & ~255ull);
    long long len = (const unsigned char*)&code_end_marker - pc;
    const u32 top = len < 16 ? 0u : len > 65536 ? 65520u : ((u32)len - 16u) & ~15u;
#pragma unroll
    for (int i = 0; i < 8; ++i) {
      const u32 o = (threadIdx.x & 511) * 16 + i * 8192;
      glds16d(pc, o < top ? o : top, (u32)(size_t)(L + L_SX));
    }
  }
  if (bid < 64) {
  const int h = xcd * 4 + (bid >> 4), hf = (bid >> 3) & 1, ac = h * 2 + hf;
  const unsigned char* ckv = sptr((const unsigned char*)p.Ac + ((size_t)hf * 8192 + h * 256) * 1024);
  if (wave >= 8) {
    const int hw = wave - 8;
    const int lane = fresh_lane();
    __builtin_amdgcn_s_setprio(3);
    if (hw == 3 && lane == 0) { *(LAS u32x4*)(L + L_SYNC) = u32x4{0u, 0u, 0u, 0u}; *(LAS u32x4*)(L + L_SYNC + 16) = u32x4{0u, 0u, 0u, 0u}; }
    const u64 tag = stage0(p, L, rs, bid, hw, lane);
    lds_add(L, L_SYNC + 12, lane);
    if (hw >= 2) {
      *(LAS u32*)(L + L_QB + (hw - 2) * 256 + lane * 4) = gather_qp(rs, 1, h, hw - 2, tag, lane);
      lds_add(L, L_SYNC, lane);
    } else if (hw == 1) {
      const u32 pk = gather_qp(rs, 0, h, hf, tag, lane), tlo = (u32)(tag << 16), thi = (u32)(tag >> 16);
      __builtin_amdgcn_raw_buffer_store_b128(u32x4{tlo | (pk & 0xffffu), thi, tlo | (pk >> 16), thi}, rs, WS_QG + (hf * 32 + h) * 1024 + lane * 16, 0, SC1);
    } else {
      const u32x4 sv = {SENT, SENT, SENT, SENT};
#pragma unroll
      for (int i = 0; i < 8; ++i) __builtin_amdgcn_raw_buffer_store_b128(sv, rs, ((i & 4) ? WS_YB : WS_YA) + ac * 4096 + (i & 3) * 1024 + lane * 16, 0, SC1);
      DRAIN();
      if (lds_add(L, L_SYNC + 4, lane) == 8u && lane == 0) __hip_atomic_store((GAS u64*)(p.ws + WS_F1) + ac, tag, RLX_AGENT);
    }
    lds_wait_ge(L, L_SYNC + 12, 4u);
    lds_wait_ge(L, L_SYNC + 4, 9u);
#pragma unroll 4
    for (int i = 0; i < 16; ++i) glds16(ckv + (64 + 4 * i + hw) * 1024, lane * 16, xbase + (64 + 4 * i + hw) * RS);
    DRAIN();
    BAR();
#pragma unroll 1
    for (int round = 0; round < 5; ++round) {
      BAR();
      if (round >= 1) {
#pragma unroll 4
        for (int i = 0; i < 8; ++i) glds16(ckv + ((round + 3) * 32 + 8 * hw + i) * 1024, lane * 16, xbase + (((round + 3) & 3) * 32 + 8 * hw + i) * RS);
      }
      asm volatile("s_waitcnt vmcnt(16)" ::: "memory");
      BAR();
    }
    u32x4 wv[16], wo[16];
    const __amdgpu_buffer_rsrc_t rv = mkrs(p.Awuv, 4194304), ro = mkrs(p.Awo, 4194304);
    const u32 vo_v = (128 * hw + (lane >> 3)) * 8192 + h * 256 + hf * 128 + (lane & 7) * 16;
    BAR();
#pragma unroll
    for (int i = 0; i < 8; ++i) wv[i] = ldnt(rv, vo_v, i * 65536);
    SCHED();
    asm volatile("s_waitcnt vmcnt(16)" ::: "memory");
    BAR();
    BAR();
#pragma unroll
    for (int i = 8; i < 16; ++i) wv[i] = ldnt(rv, vo_v, i * 65536);
    SCHED();
    asm volatile("s_waitcnt vmcnt(16)" ::: "memory");
    BAR();
    BAR();
    BAR();
    BAR();
#pragma unroll
    for (int i = 0; i < 16; ++i) wo[i] = ldnt(ro, lane * 16 + (i & 3) * 1024, (h * 128 + 64 * hf + 16 * hw + (i & ~3)) * 1024);
    SCHED();
#pragma unroll
    for (int i = 0; i < 16; ++i) *(LAS u32x4*)(L + L_X + (16 * hw + i) * 1024 + lane * 16) = wv[i];
    BAR();
#pragma unroll
    for (int i = 0; i < 16; ++i) *(LAS u32x4*)(L + L_WO + (16 * hw + i) * RS + lane * 16) = wo[i];
    BAR();
    BAR();
    BAR();
    if (hw == 1) {
      for (u32 spins = 0; spins < SPIN_CAP; ++spins) {
        const int i6 = (lane & 7) > 5 ? 5 : (lane & 7);
        if (__all(__hip_atomic_load((GAS u64*)(p.ws + WS_FC) + h * 8 + i6, RLX_AGENT) == tag)) break;
        __builtin_amdgcn_s_sleep(4);
      }
    }
    BAR();
    if (hw == 1 && bid == 0) {
      const int i6 = (lane & 7) > 5 ? 5 : (lane & 7);
      for (u32 spins = 0; spins < SPIN_CAP; ++spins) {
        bool ok = true;
#pragma unroll
        for (int i = 0; i < 4; ++i) ok &= (__hip_atomic_load((GAS u64*)(p.ws + WS_FC) + i * 64 + (lane & ~7) + i6, RLX_AGENT) == tag);
        if (__all(ok)) break;
        __builtin_amdgcn_s_sleep(4);
      }
      if (lane == 0) __hip_atomic_store((GAS u64*)(p.ws + WS_EPOCH), tag, RLX_AGENT);
    }
    return;
  }
  {
    const int w = wave;
    u64 tag;
    {
      const int lane = fresh_lane(), r = lane >> 4, c = lane & 15;
      __builtin_amdgcn_s_barrier();
      u32x4 wu[8];
      {
        const __amdgpu_buffer_rsrc_t ru = mkrs(p.Awuk, 4194304);
#pragma unroll
        for (int i = 0; i < 8; ++i) wu[i] = ldnt(ru, (256 * hf + 32 * w + c) * 8192 + h * 256 + r * 16 + (i & 3) * 64, (i >> 2) * 131072);
      }
      tag = read_tag(p.ws);
      PIN4(wu, 0); PIN4(wu, 4);
#pragma unroll 4
      for (int i = 0; i < 8; ++i) {
        asm volatile("s_waitcnt vmcnt(2)" ::: "memory");
        glds16(ckv + (8 * i + w) * 1024, lane * 16, xbase + (8 * i + w) * RS);
      }
      lds_wait_ge(L, L_SYNC, 2u);
      u32x4 qa[4];
#pragma unroll
      for (int kk = 0; kk < 4; ++kk) qa[kk] = *(LAS u32x4*)(L + L_QB + (c & 1) * 256 + kk * 64 + r * 16);
      f32x4 d0 = {0.f, 0.f, 0.f, 0.f}, d1 = d0;
#pragma unroll
      for (int kk = 0; kk < 4; ++kk) { d0 = mfma16(wu[kk], qa[kk], d0); d1 = mfma16(wu[4 + kk], qa[kk], d1); }
      const int pt = (c >> 1) & 1, tl = (c >> 2) & 1;
      const f32x4 d = tl ? d1 : d0;
      const u32 h01 = cvtpk(d[0], d[1]), h23 = cvtpk(d[2], d[3]);
      const u32 l01 = cvtpk(d[0] - bflo(h01), d[1] - bfhi(h01)), l23 = cvtpk(d[2] - bflo(h23), d[3] - bfhi(h23));
      if (c < 8) __builtin_amdgcn_raw_buffer_store_b64(pt ? u32x2{l01, l23} : u32x2{h01, h23}, rs,
                                                       WS_QX + (((c & 1) * 32 + h) * 2 + pt) * 1024 + (256 * hf + 32 * w + 16 * tl + 4 * r) * 2, 0, SC1);
      DRAIN();
      if (lds_add(L, L_SYNC + 4, lane) == 8u && lane == 0) __hip_atomic_store((GAS u64*)(p.ws + WS_F1) + ac, tag, RLX_AGENT);
      if (w == 0) { poll_flags<1, 63, 4>(p.ws + WS_F1, tag, lane); lds_set(L, L_SYNC + 8, lane); }
      else lds_wait_ge(L, L_SYNC + 8, 1u);
    }
    const int cq = w & 3, C = w >> 2;
    u32x4 qf[8];
    {
      const int lane = fresh_lane(), r = lane >> 4, c = lane & 15;
#pragma unroll
      for (int i = 0; i < 8; ++i)
        qf[i] = ldsc(rs, WS_QX + (hf * 32 + 16 * C + c) * 2048 + 256 * cq + 16 * r + (i >> 2) * 1024 + (i & 3) * 64, 0);
      PIN4(qf, 0); PIN4(qf, 4);
    }
    f32x4 acc[4][2];
#pragma unroll
    for (int i = 0; i < 4; ++i) { acc[i][0] = f32x4{0.f, 0.f, 0.f, 0.f}; acc[i][1] = f32x4{0.f, 0.f, 0.f, 0.f}; }
    float mrun = -INFINITY, lrun = 0.f;
    BAR();
    {
      const int lane = fresh_lane(), r = lane >> 4, c = lane & 15;
#pragma unroll 1
      for (int round = 0; round < 8; ++round) {
        LAS unsigned char* xs = L + L_X + (round & 3) * (32 * RS);
        {
          u32x4 a[8];
#pragma unroll
          for (int i = 0; i < 8; ++i) a[i] = *(LAS u32x4*)(xs + (16 * (i >> 2) + c) * RS + 256 * cq + (i & 3) * 64 + r * 16);
          f32x4 s0 = {0.f, 0.f, 0.f, 0.f}, s1 = s0;
#pragma unroll
          for (int kk = 0; kk < 4; ++kk) {
            s0 = mfma16(a[kk], qf[kk], s0); s1 = mfma16(a[4 + kk], qf[kk], s1);
            s0 = mfma16(a[kk], qf[4 + kk], s0); s1 = mfma16(a[4 + kk], qf[4 + kk], s1);
          }
          *(LAS f32x4*)(L + L_SX + (cq * 32 + 16 * C + c) * 144 + (4 * r) * 4) = s0;
          *(LAS f32x4*)(L + L_SX + (cq * 32 + 16 * C + c) * 144 + (16 + 4 * r) * 4) = s1;
        }
        BAR();
        {
          const int hd = 4 * w + (lane & 3), kg = lane >> 2;
          LAS unsigned char* sr = L + L_SX + hd * 144 + kg * 8;
          const f32x2 sa = (*(LAS f32x2*)(sr) + *(LAS f32x2*)(sr + 4608)) + (*(LAS f32x2*)(sr + 2 * 4608) + *(LAS f32x2*)(sr + 3 * 4608));
          const u32 r01 = cvtpk(sa[0], sa[1]);
          const u32 t01 = cvtpk(bflo(r01) * SCALE, bfhi(r01) * SCALE);
          float v0 = bflo(t01), v1 = bfhi(t01);
          float mx = fmaxf(v0, v1);
          mx = fmaxf(mx, dpp<ROR4>(mx)); mx = fmaxf(mx, dpp<ROR8>(mx));
          mx = fmaxf(xrow_max(mx), mrun);
          const float alpha = __builtin_amdgcn_exp2f((mrun - mx) * LOG2E);
          mrun = mx;
          v0 = __builtin_amdgcn_exp2f((v0 - mx) * LOG2E); v1 = __builtin_amdgcn_exp2f((v1 - mx) * LOG2E);
          lrun = lrun * alpha + (v0 + v1);
          const int key = 2 * kg, pos = ((key >> 2) & 3) * 8 + (key >> 4) * 4 + (key & 3);
          *(LAS u32*)(L + L_P + hd * 80 + pos * 2) = cvtpk(v0, v1);
          if (kg == 0) *(LAS float*)(L + L_AL + hd * 4) = alpha;
        }
        BAR();
        {
          const u32x4 pf0 = *(LAS u32x4*)(L + L_P + c * 80 + r * 16), pf1 = *(LAS u32x4*)(L + L_P + (16 + c) * 80 + r * 16);
          const float al0 = *(LAS float*)(L + L_AL + c * 4), al1 = *(LAS float*)(L + L_AL + 64 + c * 4);
          LAS unsigned char* tb = xs + (4 * r + (c >> 2)) * RS + 128 * w + (c & 3) * 8;
          u32x2 a0[4], a1[4];
#pragma unroll
          for (int ct = 0; ct < 4; ++ct) { a0[ct] = trread(tb + ct * 32); a1[ct] = trread(tb + 16 * RS + ct * 32); }
#pragma unroll
          for (int ct = 0; ct < 4; ++ct) {
            const u32x4 a = {a0[ct][0], a0[ct][1], a1[ct][0], a1[ct][1]};
            acc[ct][0] = mfma16(a, pf0, acc[ct][0] * al0);
            acc[ct][1] = mfma16(a, pf1, acc[ct][1] * al1);
          }
        }
      }
#pragma unroll
      for (int cp2 = 0; cp2 < 2; ++cp2)
#pragma unroll
        for (int CC = 0; CC < 2; ++CC) {
          const f32x4 a = acc[2 * cp2][CC], bb = acc[2 * cp2 + 1][CC];
          const u32x4 v = {cvtpk(a[0], a[1]), cvtpk(a[2], a[3]), cvtpk(bb[0], bb[1]), cvtpk(bb[2], bb[3])};
          __builtin_amdgcn_raw_buffer_store_b128(v, rs, WS_PART + ((hf * 32 + 16 * CC + c) * 32 + h) * 1024 + w * 128 + cp2 * 64 + r * 16, 0, SC1);
        }
      float lt = lrun + dpp<ROR4>(lrun); lt += dpp<ROR8>(lt); lt = xrow_sum(lt);
      if (lane < 4) __builtin_amdgcn_raw_buffer_store_b64(u32x2{asu(mrun), asu(lt)}, rs, WS_PST + ((hf * 32 + 4 * w + lane) * 32 + h) * 8, 0, SC1);
      DRAIN();
      BAR();
      if (w == 0 && lane == 0) __hip_atomic_store((GAS u64*)(p.ws + WS_F2) + ac, tag, RLX_AGENT);
      u32x4 wb[8];
      if (w != 0) {
        const __amdgpu_buffer_rsrc_t rb = mkrs(p.Bwo, 4194304);
#pragma unroll
        for (int i = 0; i < 8; ++i) wb[i] = ldnt(rb, lane * 16 + (i & 3) * 1024, (h * 128 + 64 * hf + 8 * w + (i & 4)) * 1024);
      } else {
        poll_flags<1, 63, 4>(p.ws + WS_F2, tag, lane);
      }
      __builtin_amdgcn_s_barrier();
      asm volatile("" ::: "memory");
      const int bb = w >> 2, s8 = 8 * (w & 3);
      {
        const u32x2 st = __builtin_amdgcn_raw_buffer_load_b64(rs, WS_PST + ((bb * 32 + h) * 32 + (lane & 31)) * 8, 0, SC1);
        u32x4 pv[8];
#pragma unroll
        for (int i = 0; i < 8; ++i) pv[i] = ldsc(rs, WS_PART + ((bb * 32 + h) * 32 + s8) * 1024 + lane * 16, i * 1024);
        if (w == 0) {
          const __amdgpu_buffer_rsrc_t rb = mkrs(p.Bwo, 4194304);
#pragma unroll
          for (int i = 0; i < 8; ++i) wb[i] = ldnt(rb, lane * 16 + (i & 3) * 1024, (h * 128 + 64 * hf + (i & 4)) * 1024);
        }
        const float ms = asf(st[0]), ls = asf(st[1]);
        const float M = xrow_max(row_max16(ms));
        const float f = __builtin_amdgcn_exp2f((ms - M) * LOG2E);
        const float cf = f / (0.5f * xrow_sum(row_sum16(f * ls)));
        float o[8];
#pragma unroll
        for (int k = 0; k < 8; ++k) o[k] = 0.f;
#pragma unroll
        for (int i = 0; i < 8; ++i) fma8p(o, pv[i], asf(__builtin_amdgcn_readlane(asu(cf), s8 + i)));
        *(LAS f32x4*)(L + L_SX + w * 2048 + lane * 32) = f32x4{o[0], o[1], o[2], o[3]};
        *(LAS f32x4*)(L + L_SX + w * 2048 + lane * 32 + 16) = f32x4{o[4], o[5], o[6], o[7]};
      }
      BAR();
      {
        const int e2 = (w & 3) * 64 + lane;
        LAS unsigned char* sr = L + L_SX + bb * 8192 + e2 * 8;
        const f32x2 x = (*(LAS f32x2*)(sr) + *(LAS f32x2*)(sr + 2048)) + (*(LAS f32x2*)(sr + 4096) + *(LAS f32x2*)(sr + 6144));
        const u32 hi = cvtpk(x[0], x[1]);
        *(LAS u32*)(L + L_XV + bb * 2048 + e2 * 4) = hi;
        *(LAS u32*)(L + L_XV + bb * 2048 + 1024 + e2 * 4) = cvtpk(x[0] - bflo(hi), x[1] - bfhi(hi));
      }
      BAR();
      if (w < 4) {
        LAS unsigned char* tb = L + L_X + (4 * r + (c >> 2)) * 128 + w * 32 + (c & 3) * 8;
        f32x4 d = {0.f, 0.f, 0.f, 0.f};
#pragma unroll
        for (int kk = 0; kk < 16; ++kk) {
          const u32x2 a0 = trread(tb + kk * 4096), a1 = trread(tb + 2048 + kk * 4096);
          const u32x4 bf = *(LAS u32x4*)(L + L_XV + (c & 1) * 2048 + ((c >> 1) & 1) * 1024 + kk * 64 + r * 16);
          d = mfma16(u32x4{a0[0], a0[1], a1[0], a1[1]}, bf, d);
        }
#pragma unroll
        for (int e = 0; e < 4; ++e) d[e] += dpp<XOR2>(d[e]);
        if (c < 2) *(LAS u32x2*)(L + L_QB + c * 256 + ((w >> 1) * 32 + r * 8 + (w & 1) * 4) * 2) = u32x2{cvtpk(d[0], d[1]), cvtpk(d[2], d[3])};
      }
      u32x4 wf[8];
#define LOAD_WF(base) do { LAS unsigned char* ab_ = L + (base) + (4 * r + (c >> 2)) * RS + (64 * w + 4 * (c & 3)) * 2; \
        _Pragma("unroll") for (int i = 0; i < 8; ++i) { const u32x2 a0 = trread(ab_ + (32 * (i & 1)) * RS + (i >> 1) * 32), a1 = trread(ab_ + (32 * (i & 1) + 16) * RS + (i >> 1) * 32); \
          wf[i] = u32x4{a0[0], a0[1], a1[0], a1[1]}; } } while (0)
#define OUT_PROJ(b0, b1, dst) do { f32x4 y_ = {0.f, 0.f, 0.f, 0.f}; _Pragma("unroll") for (int tt = 0; tt < 4; ++tt) { \
          f32x4 d_ = mfma16(wf[2 * tt], b0, f32x4{0.f, 0.f, 0.f, 0.f}); d_ = mfma16(wf[2 * tt + 1], b1, d_); y_ = (((c >> 1) & 3) == tt) ? d_ : y_; } \
        if (c < 8) stsc(san4(y_), rs, (dst) + ac * 4096 + (c & 1) * 2048 + (64 * w + 16 * ((c >> 1) & 3) + 4 * r) * 4); } while (0)
      LOAD_WF(L_WO);
      BAR();
      {
        const u32x4 b0 = *(LAS u32x4*)(L + L_QB + (c & 1) * 256 + r * 16), b1 = *(LAS u32x4*)(L + L_QB + (c & 1) * 256 + 64 + r * 16);
        OUT_PROJ(b0, b1, WS_YA);
      }
#pragma unroll
      for (int i = 0; i < 8; ++i) *(LAS u32x4*)(L + L_X + (8 * w + i) * RS + lane * 16) = wb[i];
      BAR();
      LOAD_WF(L_X);
      PIN4(wf, 0); PIN4(wf, 4);
      {
        constexpr int NSLOT = 3 + 128 - 3 * NSS;
        const int pr = bb * 32 + h, q4 = w & 3, j0 = q4 + 4 * r, j1r = q4 + 16 + 4 * r;
        const bool v1 = j1r < NSLOT;
        const int j1 = j1r < NSLOT ? j1r : j0;
        const u32 so = WS_OBS + (pr * 32 + (c < 8 ? j0 : j1)) * 8;
        for (u32 spins = 0; spins < SPIN_CAP; ++spins) {
          const u32 v = __builtin_amdgcn_raw_buffer_load_b32(rs, so, 0, SC1);
          if (__builtin_popcountll(__ballot(v != SENT)) >= 40) break;
          __builtin_amdgcn_s_sleep(12);
          asm volatile("" ::: "memory");
        }
        u32x4 y0, y1; u32x2 st;
        for (u32 spins = 0; spins < SPIN_CAP; ++spins) {
          y0 = ldsc(rs, WS_OB + (pr * 32 + j0) * 512 + (64 * hf + 4 * c) * 4, 0);
          y1 = ldsc(rs, WS_OB + (pr * 32 + j1) * 512 + (64 * hf + 4 * c) * 4, 0);
          st = __builtin_amdgcn_raw_buffer_load_b64(rs, so, 0, SC1);
          if (__all(valid4(y0) && valid4(y1) && st[0] != SENT && st[1] != SENT)) break;
          asm volatile("" ::: "memory");
        }
        const float ms = asf(st[0]), ls = asf(st[1]), mo = dpp<ROR8>(ms), lo_ = dpp<ROR8>(ls);
        const float m0 = c < 8 ? ms : mo, l0 = c < 8 ? ls : lo_, m1 = c < 8 ? mo : ms, l1 = c < 8 ? lo_ : ls;
        const float M = xrow_max(fmaxf(m0, m1));
        const float c0 = __builtin_amdgcn_exp2f((m0 - M) * LOG2E), c1 = v1 ? __builtin_amdgcn_exp2f((m1 - M) * LOG2E) : 0.f;
        f32x4 o = c0 * __builtin_bit_cast(f32x4, y0) + c1 * __builtin_bit_cast(f32x4, y1);
#pragma unroll
        for (int e = 0; e < 4; ++e) o[e] = xrow_sum(o[e]);
        const float Lw = xrow_sum(c0 * l0 + c1 * l1);
        if (r == 0) *(LAS f32x4*)(L + L_SX + w * 256 + c * 16) = o;
        if (lane == 16) *(LAS u32x2*)(L + L_SX + 2048 + w * 8) = u32x2{asu(M), asu(Lw)};
        lds_add(L, L_SYNC + 16, lane);
        lds_wait_ge(L, L_SYNC + 16, 8u);
        const f32x4 s0 = *(LAS f32x4*)(L + L_SX + 2048 + (c & 1) * 32), s1 = *(LAS f32x4*)(L + L_SX + 2048 + (c & 1) * 32 + 16);
        const float Mb = fmaxf(fmaxf(s0[0], s0[2]), fmaxf(s1[0], s1[2]));
        float f0 = __builtin_amdgcn_exp2f((s0[0] - Mb) * LOG2E), f1 = __builtin_amdgcn_exp2f((s0[2] - Mb) * LOG2E);
        float f2 = __builtin_amdgcn_exp2f((s1[0] - Mb) * LOG2E), f3 = __builtin_amdgcn_exp2f((s1[2] - Mb) * LOG2E);
        const float inv = 1.f / ((f0 * s0[1] + f1 * s0[3]) + (f2 * s1[1] + f3 * s1[3]));
        f0 *= inv; f1 *= inv; f2 *= inv; f3 *= inv;
        u32 ob[8];
#pragma unroll
        for (int i = 0; i < 4; ++i) {
          LAS unsigned char* sr = L + L_SX + (c & 1) * 1024 + (16 * i + 4 * r) * 4;
          const f32x4 x = (f0 * *(LAS f32x4*)(sr) + f1 * *(LAS f32x4*)(sr + 256)) + (f2 * *(LAS f32x4*)(sr + 512) + f3 * *(LAS f32x4*)(sr + 768));
          ob[2 * i] = cvtpk(x[0], x[1]); ob[2 * i + 1] = cvtpk(x[2], x[3]);
        }
        const u32x4 b0 = {ob[0], ob[1], ob[2], ob[3]}, b1 = {ob[4], ob[5], ob[6], ob[7]};
        OUT_PROJ(b0, b1, WS_YB);
      }
      if (w == 0) final_reduce<0>(p, rs, WS_YB, hf, h, 512, lane);
      if (w == 1) final_reduce<0>(p, rs, WS_YA, hf, h, 0, lane);
    }
    return;
  }
  }
  {
    const int k24 = (bid - 64) >> 3, s = k24 % 3, i8 = k24 / 3;
    const int x0 = (xcd - (s == 0 ? 0 : s == 1 ? 1 + i8 % 3 : 4 + i8 % 3)) & 7;
    const int h = x0 * 4 + (i8 >> 1), b = i8 & 1, i6 = b * 3 + s;
    if (wave >= NBW) {
      const int hw = wave - 8, lane = fresh_lane();
      __builtin_amdgcn_s_setprio(3);
      if (hw == 0 && lane == 0) { *(LAS u32x4*)(L + L_SYNC) = u32x4{0u, 0u, 0u, 0u}; *(LAS u32x4*)(L + L_SYNC + 16) = u32x4{0u, 0u, 0u, 0u}; }
      const u64 tag = stage0(p, L, rs, bid, hw, lane);
      if (hw == 0) {
        *(LAS u32*)(L + L_QB + lane * 4) = gather_qp(rs, 0, h, b, tag, lane);
        lds_set(L, L_SYNC, lane);
        __builtin_amdgcn_s_setprio(1);
        constexpr int NL = 2 * NSS;
        constexpr int NBK = NL / LOCK, NBV = (NL - LOCK - WIN) / LOCK;
        const u32 cto = lane == 0 ? WS_CTR + i8 * 256 : WS_END;
#pragma unroll 1
        for (int i = 0; i < NBK; ++i) __builtin_amdgcn_s_barrier();
        {
          const u32 qo = WS_QG + (b * 32 + (i8 >> 1)) * 1024, tlo = (u32)(tag << 16), thi = (u32)(tag >> 16);
          GAS u64* fcp = (GAS u64*)(p.ws + WS_FC) + (((lane >> 2) & 7) * 4 + (i8 >> 1)) * 8 + b * 3 + ((lane & 3) == 3 ? 0 : (lane & 3));
          u32x4 g[8];
#pragma unroll
          for (int i = 0; i < 8; ++i) g[i] = ldsc(rs, qo + lane * 16, i * 4096);
          const u64 fcv = __hip_atomic_load(fcp, RLX_AGENT);
          SCHED();
          asm volatile("" ::: "memory");
          __builtin_amdgcn_s_barrier(); __builtin_amdgcn_s_barrier(); __builtin_amdgcn_s_barrier();
          asm volatile("" ::: "memory");
          SCHED();
          bool ok = true;
#pragma unroll
          for (int i = 0; i < 8; ++i) {
            ok &= (g[i][0] & 0xffff0000u) == tlo && g[i][1] == thi && (g[i][2] & 0xffff0000u) == tlo && g[i][3] == thi;
            *(LAS u32*)(L + L_P + i * 256 + lane * 4) = (g[i][0] & 0xffffu) | (g[i][2] << 16);
          }
          if (!__all(ok)) gather_q<8>(L + L_P, rs, qo, 4096, tag, lane);
          if (!__all(fcv == tag)) { for (u32 sp = 0; sp < SPIN_CAP; ++sp) { if (__all(__hip_atomic_load(fcp, RLX_AGENT) == tag)) break; } }
          LDSWAIT();
        }
#pragma unroll 1
        for (int i = 0; i < NBV - 5; ++i) __builtin_amdgcn_s_barrier();
        u32 tka = 0, tkb = 0;
        TICKET(tka);
        __builtin_amdgcn_s_barrier();
        TICKET(tkb);
        __builtin_amdgcn_s_barrier();
        dyn_side(L, rs, lane, i8, cto, tka, tkb, (u32)(b * 32 + h), (u32)s);
      }
      if (hw == 1) {
        const u32x4 sv = {SENT, SENT, SENT, SENT};
#pragma unroll
        for (int i = 0; i < 5; ++i) __builtin_amdgcn_raw_buffer_store_b128(sv, rs, WS_OB + ((b * 32 + h) * 32 + 10 * s) * 512 + i * 1024 + lane * 16, 0, SC1);
        if (lane < 10) __builtin_amdgcn_raw_buffer_store_b64(u32x2{SENT, SENT}, rs, WS_OBS + ((b * 32 + h) * 32 + 10 * s + lane) * 8, 0, SC1);
        DRAIN();
        lds_wait_ge(L, L_SYNC, 1u);
        if (lane == 0) __hip_atomic_store((GAS u64*)(p.ws + WS_FC) + h * 8 + i6, tag, RLX_AGENT);
      }
      return;
    }
    {
      static_assert(3 + 128 - 3 * NSS <= 30 && 3 + 128 - 3 * NSS >= 16, "output slots");
      constexpr int NL = 2 * NSS, NG = (NL + 15) / 16, RL = NL - 16 * (NG - 1);
      const int lane = fresh_lane(), c = lane & 15, w = wave;
      const size_t pairoff = (size_t)(b * 32 + h) * 2097152;
      const __amdgpu_buffer_rsrc_t kb = mkrs((const unsigned char*)p.Bk + pairoff, 2097152);
      const __amdgpu_buffer_rsrc_t vb = mkrs((const unsigned char*)p.Bv + pairoff, 2097152);
      const u32 kvo = s * 16384 + w * 2048 + lane * 16;
#define LDKV(rs_, t) ldnt(rs_, kvo + ((t) & 1) * 1024, ((t) >> 1) * 49152)
      u32x4 ring[PRE];
#pragma unroll
      for (int t = 0; t < EARLY; ++t) ring[t] = LDKV(kb, t);
      SCHED();
      __builtin_amdgcn_s_barrier();
      SCHED();
#pragma unroll
      for (int t = EARLY; t < PRE; ++t) { if (t > WIN) PACE(); ring[t] = LDKV(kb, t); SCHED(); }
      lds_wait_ge(L, L_SYNC, 1u);
      const u32x4 q = *(LAS u32x4*)(L + L_QB + c * 16);
#define ISS(t) ((t) < 0 ? PRE : ((t) + WIN + 1 > PRE + ((t) + 1) / 2 ? (t) + WIN + 1 : PRE + ((t) + 1) / 2))
      static_assert(ISS(NL - 1) == NL + WIN, "catch-up must finish inside the K phase");
      float sc[NG];
#pragma unroll
      for (int g = 0; g < NG; ++g) {
        float x[16];
#pragma unroll
        for (int u = 0; u < 16; ++u) {
          const int t = g * 16 + u;
          if (t < NL) {
            if (ISS(t) > ISS(t - 1)) {
              const int tn = ISS(t) - 1;
              if (tn > t + WIN) PACE();
              if (tn < NL) ring[tn % PRE] = LDKV(kb, tn); else ring[tn % PRE] = LDKV(vb, tn - NL);
            }
            SCHED();
            const u32x4 kv = ring[t % PRE];
            float d = dot2(kv[0], q[0], 0.f); d = dot2(kv[1], q[1], d); d = dot2(kv[2], q[2], d); d = dot2(kv[3], q[3], d);
            asm volatile("" : "+v"(d) :: "memory");
            x[u] = d;
            if ((t % LOCK) == LOCK - 1) __builtin_amdgcn_s_barrier();
            SCHED();
          } else x[u] = 0.f;
        }
        const float raw = tr_reduce16(x, c);
        sc[g] = rbf(rbf(raw) * SCALE);
        if (g == NG - 1 && RL < 16) sc[g] = (c < RL) ? sc[g] : -INFINITY;
        asm volatile("" : "+v"(sc[g]) :: "memory");
      }
      float mw = sc[0];
#pragma unroll
      for (int g = 1; g < NG; ++g) mw = fmaxf(mw, sc[g]);
      mw = xrow_max(row_max16(mw));
      float lrow = 0.f;
#pragma unroll
      for (int g = 0; g < NG; ++g) { sc[g] = __builtin_amdgcn_exp2f((sc[g] - mw) * LOG2E); lrow += sc[g]; }
      lrow = row_sum16(lrow);
      float acc[8];
#pragma unroll
      for (int k = 0; k < 8; ++k) acc[k] = 0.f;
      u32 va = 0, vb_ = 0;
      u32x4 ua[4], ub[4];
      const __amdgpu_buffer_rsrc_t kg = mkrs(p.Bk, 134217728u), vg = mkrs(p.Bv, 134217728u);
#pragma unroll
      for (int g = 0; g < NG; ++g) {
#define VSTEP(u) { const int t = g * 16 + u; if (t < NL) { const int tn = t + WIN; if (tn < NL) ring[(NL + tn) % PRE] = LDKV(vb, tn); \
          if (t == NL - WIN) UNIT_ISSUE(ua, va, 0); \
          SCHED(); \
          const u32x4 vv = ring[(NL + t) % PRE]; fma8<u>(acc, vv, sc[g]); \
          asm volatile("" : "+v"(acc[0]), "+v"(acc[1]), "+v"(acc[2]), "+v"(acc[3]), "+v"(acc[4]), "+v"(acc[5]), "+v"(acc[6]), "+v"(acc[7]) :: "memory"); \
          if ((t % LOCK) == LOCK - 1 && t < NL - LOCK - WIN) __builtin_amdgcn_s_barrier(); SCHED(); } }
        VSTEP(0) VSTEP(1) VSTEP(2) VSTEP(3) VSTEP(4) VSTEP(5) VSTEP(6) VSTEP(7)
        VSTEP(8) VSTEP(9) VSTEP(10) VSTEP(11) VSTEP(12) VSTEP(13) VSTEP(14) VSTEP(15)
#undef VSTEP
      }
#undef LDKV
#undef ISS
      UNIT_ISSUE(ub, vb_, 1);
      dyn_flush(L, acc, mw, lrow, 1, w, lane);
      DYN_LOOP();
      asm volatile("" :: "v"(ua[0]), "v"(ua[1]), "v"(ua[2]), "v"(ua[3]), "v"(ub[0]), "v"(ub[1]), "v"(ub[2]), "v"(ub[3]) : "memory");
      return;
    }
  }
}

__device__ __attribute__((noinline, used)) void code_end_marker() { asm volatile("s_nop 0"); }

extern "C" void kernel_launch(void* const* d_in, const int* in_sizes, int n_in, void* d_out, int out_size, void* d_ws, size_t ws_size,
                              hipStream_t stream) {
  Params p;
  p.Ax = (const u16*)d_in[0]; p.Ac = (const u16*)d_in[1]; p.Awq = (const u16*)d_in[2]; p.Awuk = (const u16*)d_in[3];
  p.Awuv = (const u16*)d_in[4]; p.Awo = (const u16*)d_in[5]; p.Bx = (const u16*)d_in[6]; p.Bk = (const u16*)d_in[7];
  p.Bv = (const u16*)d_in[8]; p.Bwq = (const u16*)d_in[9]; p.Bwo = (const u16*)d_in[10];
  p.out = (u16*)d_out; p.ws = (unsigned char*)d_ws;
  fused_decode_kernel<<<NWG, NTHR, 0, stream>>>(p);
}
```
